```python
import torch

def _softmax(x, axis=-1):
    return torch.softmax(x, dim=axis)
def _gelu(x):
    return torch.nn.functional.gelu(x, approximate="tanh")
def _silu(x):
    return torch.nn.functional.silu(x)
def _sigmoid(x):
    return torch.sigmoid(x)
def _layernorm(x, axis=-1, eps=1e-5):
    m = x.mean(dim=axis, keepdim=True)
    v = x.var(dim=axis, keepdim=True, unbiased=False)
    return (x - m) / torch.sqrt(v + eps)
def _scatter_add(tgt, idx, src, axis=0):
    return tgt.clone().index_add_(axis, idx, src)
def _segment_sum(src, seg, n):
    out = torch.zeros((n,) + src.shape[1:], dtype=src.dtype)
    return out.index_add_(0, seg, src)
def _pad(x, pw):
    flat = tuple(v for pair in reversed(pw) for v in pair)
    return torch.nn.functional.pad(x, flat)

kernel_name = 'standalone__weight_only_int_quip__decode'

def setup_inputs():

    _w = (lambda a: (lambda t: t.to(torch.bfloat16) if t.is_floating_point() else t)(torch.as_tensor(a)))
    B, S, D, F = 4, 1, 576, 4608
    G = 32 if 32 > 0 else D
    D = (D // G) * G
    ng = D // G
    bits = 4 if 4 > 0 else 4
    qmax = 1 << bits
    qw = torch.randint(0, qmax, (D, F)).to(torch.int32)
    U, _ = (lambda *_a, **_k: torch.linalg.qr(*[torch.as_tensor(_x).float() for _x in _a], **_k))((lambda *s: torch.randn(*s, dtype=torch.bfloat16) * 0.05)(D, D))
    V, _ = (lambda *_a, **_k: torch.linalg.qr(*[torch.as_tensor(_x).float() for _x in _a], **_k))((lambda *s: torch.randn(*s, dtype=torch.bfloat16) * 0.05)(F, F))
    return {
        "x": _w((lambda *s: torch.randn(*s, dtype=torch.bfloat16) * 0.05)(B, S, D)),
        "qw": _w(qw),
        "scales": _w(torch.abs((lambda *s: torch.randn(*s, dtype=torch.bfloat16) * 0.05)(ng, F)) * 0.02 + 0.005),
        "zeros": _w(torch.full((ng, F), qmax / 2.0, dtype=float)),
        "U": _w(U.to(float)),
        "V": _w(V.to(float)),
    }

def reference(x, qw, scales, zeros, U, V):
    B, S, D = x.shape
    ng, F = scales.shape
    G = D // ng
    s = (lambda x, n, axis: torch.repeat_interleave(x, n, dim=axis))(scales, G, axis=0)
    z = (lambda x, n, axis: torch.repeat_interleave(x, n, dim=axis))(zeros, G, axis=0)
    wq = ((lambda x, dt: x.to(dt))(qw, x.dtype) - z) * s
    xr = torch.einsum("bsd,de->bse", x, U)
    h  = torch.einsum("bse,ef->bsf", xr, wq)
    y  = torch.einsum("bsf,fg->bsg", h, V)
    return y
```

```cpp
#include <hip/hip_runtime.h>
#include <hip/hip_bf16.h>

typedef unsigned short u16;
typedef unsigned int u32;
typedef u32 v4u __attribute__((ext_vector_type(4)));
typedef int v4i __attribute__((ext_vector_type(4)));
typedef u32 v2u __attribute__((ext_vector_type(2)));

constexpr int B = 4, D = 576, F = 4608, G = 32, NG = 18;

__device__ __forceinline__ float bf2f(u32 b) { return __builtin_bit_cast(float, b << 16); }
__device__ __forceinline__ u16 f2bf(float f) { __bf16 h = (__bf16)f; return __builtin_bit_cast(u16, h); }
__device__ __forceinline__ float rbf(float f) { return bf2f(f2bf(f)); }

__global__ void __launch_bounds__(256) k1(const u16* __restrict__ x, const u16* __restrict__ U, u16* __restrict__ xr) {
    __shared__ float red[4][4][64];
    int lane = threadIdx.x & 63, w = threadIdx.x >> 6;
    int e = blockIdx.x * 64 + lane;
    float acc[4] = {0, 0, 0, 0};
    for (int d = w; d < D; d += 4) {
        float u = bf2f(U[d * D + e]);
        for (int b = 0; b < 4; ++b) acc[b] += bf2f(x[b * D + d]) * u;
    }
    for (int b = 0; b < 4; ++b) red[w][b][lane] = acc[b];
    __syncthreads();
    int b = w;
    float s = red[0][b][lane] + red[1][b][lane] + red[2][b][lane] + red[3][b][lane];
    xr[b * D + e] = f2bf(s);
}

__global__ void __launch_bounds__(256) k2(const u16* __restrict__ xr, const int* __restrict__ qw, const u16* __restrict__ sc,
                                          const u16* __restrict__ zr, u16* __restrict__ h) {
    __shared__ float red[4][4][64];
    int lane = threadIdx.x & 63, w = threadIdx.x >> 6;
    int f = blockIdx.x * 64 + lane;
    float acc[4] = {0, 0, 0, 0};
    for (int k = w; k < D; k += 4) {
        int g = k / G;
        float q = rbf((float)qw[k * F + f]);
        float wv = rbf(rbf(q - bf2f(zr[g * F + f])) * bf2f(sc[g * F + f]));
        for (int b = 0; b < 4; ++b) acc[b] += bf2f(xr[b * D + k]) * wv;
    }
    for (int b = 0; b < 4; ++b) red[w][b][lane] = acc[b];
    __syncthreads();
    int b = w;
    float s = red[0][b][lane] + red[1][b][lane] + red[2][b][lane] + red[3][b][lane];
    h[b * F + f] = f2bf(s);
}

__global__ void __launch_bounds__(1024) k3(const u16* __restrict__ h, const u16* __restrict__ V, u16* __restrict__ y) {
    __shared__ float red[16][4][64];
    int lane = threadIdx.x & 63, w = threadIdx.x >> 6;
    int g = blockIdx.x * 64 + lane;
    float acc[4] = {0, 0, 0, 0};
    for (int f = w; f < F; f += 16) {
        float v = bf2f(V[(size_t)f * F + g]);
        for (int b = 0; b < 4; ++b) acc[b] += bf2f(h[b * F + f]) * v;
    }
    for (int b = 0; b < 4; ++b) red[w][b][lane] = acc[b];
    __syncthreads();
    if (w < 4) {
        int b = w;
        float s = 0;
        for (int i = 0; i < 16; ++i) s += red[i][b][lane];
        y[b * F + g] = f2bf(s);
    }
}

extern "C" void kernel_launch(void* const* d_in, const int* in_sizes, int n_in,
                              void* d_out, int out_size, void* d_ws, size_t ws_size,
                              hipStream_t stream) {
    const u16* x = (const u16*)d_in[0];
    const int* qw = (const int*)d_in[1];
    const u16* sc = (const u16*)d_in[2];
    const u16* zr = (const u16*)d_in[3];
    const u16* U = (const u16*)d_in[4];
    const u16* V = (const u16*)d_in[5];
    u16* xr = (u16*)d_ws;
    u16* h = xr + 4096;
    k1<<<D / 64, 256, 0, stream>>>(x, U, xr);
    k2<<<F / 64, 256, 0, stream>>>(xr, qw, sc, zr, h);
    k3<<<F / 64, 1024, 0, stream>>>(h, V, (u16*)d_out);
}
```

```cpp
#include <hip/hip_runtime.h>

typedef unsigned short u16;
typedef unsigned int u32;
typedef unsigned long long u64;
typedef u32 v4u __attribute__((ext_vector_type(4)));
typedef u32 v2u __attribute__((ext_vector_type(2)));
typedef float f2 __attribute__((ext_vector_type(2)));
typedef float f4 __attribute__((ext_vector_type(4)));
typedef float f16v __attribute__((ext_vector_type(16)));
typedef __bf16 bf2 __attribute__((ext_vector_type(2)));
typedef __bf16 bf8 __attribute__((ext_vector_type(8)));
typedef short s4 __attribute__((ext_vector_type(4)));
#define LAS __attribute__((address_space(3)))

#define A_N0 4
#define A_W0 2
#define A_W1 5
#define B_N0 4
#define B_W0 2
#define B_W1 8
#define WIN_Q 10
#define K_A 30
#define K_B 46
#define ROUND_Q 1

#define CODE_LOADS 3
#define CODE_BYTES 17664

namespace {
constexpr int D = 576, F = 4608;
constexpr int NWG = 252, NMEM = 14;
constexpr int RA = 272, RB = 432, LA = RA / 8, LB = RB / 8;
constexpr u32 SENT = 0xFFFFFFFFu;
constexpr int WS_E = 0, WS_ACK = 4096, WS_H = 40960, WS_XR = 81920, XR_REP = 9216 + 1024, WS_YP = 196608, WS_BYTES = WS_YP + NWG * 4096;
constexpr int OOB = 0x40000000;
constexpr int HS = 1040, XS = 1168;
constexpr int L_FLAG = 0, L_H = 64, L_XR = L_H + 4 * HS, L_PART = L_XR + 4 * XS, L_SLAB = L_PART + 12 * 512, L_BYTES = L_SLAB + 4 * 4096;
enum { F_RED = 2, F_XR = 3, F_H = 4, F_ISS = 5 };
constexpr u32 G_SPINS = 1u << 15, L_SPINS = 1u << 21;
constexpr int AUX_NT = 2, AUX_SC1 = 16;

typedef __amdgpu_buffer_rsrc_t rsrc_t;
__device__ __forceinline__ rsrc_t mk(const void* p, int bytes) { return __builtin_amdgcn_make_buffer_rsrc((void*)p, 0, bytes, 0x00020000); }
#define SB() __builtin_amdgcn_sched_barrier(0)
#define WAIT_VM(n) asm volatile("s_waitcnt vmcnt(%0)" ::"i"(n) : "memory")
#define WAIT_LGKM() asm volatile("s_waitcnt lgkmcnt(0)" ::: "memory")

__device__ __forceinline__ u32 lds_ld(LAS unsigned char* l, int w) { return __hip_atomic_load((LAS u32*)l + w, __ATOMIC_RELAXED, __HIP_MEMORY_SCOPE_WORKGROUP); }
__device__ __forceinline__ void lds_st(LAS unsigned char* l, int w, u32 v) { __hip_atomic_store((LAS u32*)l + w, v, __ATOMIC_RELAXED, __HIP_MEMORY_SCOPE_WORKGROUP); }
__device__ __forceinline__ void lds_wait(LAS unsigned char* l, int w, u32 target) {
#pragma nounroll
    for (u32 i = 0; i < L_SPINS; ++i) {
        if (__builtin_amdgcn_readfirstlane(lds_ld(l, w)) >= target) break;
        __builtin_amdgcn_s_sleep(1);
    }
    asm volatile("" ::: "memory");
}
__device__ __forceinline__ u32 pk_bf16(float a, float b) {
    bf2 r = __builtin_convertvector(f2{a, b}, bf2);
    return __builtin_bit_cast(u32, r);
}
__device__ __forceinline__ float lo_f(u32 v) { return __builtin_bit_cast(float, v << 16); }
__device__ __forceinline__ float hi_f(u32 v) { return __builtin_bit_cast(float, v & 0xFFFF0000u); }
__device__ __forceinline__ u32 umin(u32 a, u32 b) { return a < b ? a : b; }
template <int CTRL> __device__ __forceinline__ u32 dpp(u32 v) { return __builtin_amdgcn_update_dpp(0u, v, CTRL, 0xF, 0xF, true); }

#define ST(i) do {} while (0)
#define STC(i) do {} while (0)
#define ST_DECL
#define ST_ARG
#define ST_PASS
#define ST_OUT() do {} while (0)

template <int L, int K, int N0, int W0, int W1>
__device__ __forceinline__ void vstream(rsrc_t rv, int voff, LAS unsigned char* lds, int hoff, int lane, f16v& acc ST_ARG) {
    constexpr int STEP = 2 * F * 2;
    v4u a[L];
#pragma unroll
    for (int i = 0; i < K; ++i) {
        a[i] = __builtin_amdgcn_raw_buffer_load_b128(rv, voff, i * STEP, AUX_NT);
        SB();
        const int W = i + 1 < N0 ? W0 : W1;
        if (i >= W - 1 && i < K - 1) {
            if (i + 1 < N0) WAIT_VM(W0 - 1); else WAIT_VM(W1 - 1);
            SB();
        }
    }
    const u32 two = (lane & 32) ? 0x0302u : 0x0100u;
    const u32 one = (lane & 1) ? ((two << 16) | 0x0C0Cu) : (0x0C0C0000u | two);
    const int jj = (lane & 7) >> 1;
    const u32 s0 = jj == 0 ? one : 0x0C0C0C0Cu, s1 = jj == 1 ? one : 0x0C0C0C0Cu, s2 = jj == 2 ? one : 0x0C0C0C0Cu, s3 = jj == 3 ? one : 0x0C0C0C0Cu;
    ST(2);
    lds_wait(lds, F_H, 1);
    ST(3);
    LAS const v2u* hp = (LAS const v2u*)(lds + hoff);
    v2u hc = hp[0], hn = hp[1];
#pragma unroll
    for (int t = 0; t < L / 2; ++t) {
        v2u hn2 = hn;
        if (t + 2 < L / 2) { hn2 = hp[t + 2]; SB(); }
        v4u A0 = {__builtin_amdgcn_perm(hc.x, hc.x, s0), __builtin_amdgcn_perm(hc.x, hc.x, s1), __builtin_amdgcn_perm(hc.x, hc.x, s2), __builtin_amdgcn_perm(hc.x, hc.x, s3)};
        acc = __builtin_amdgcn_mfma_f32_32x32x16_bf16(__builtin_bit_cast(bf8, A0), __builtin_bit_cast(bf8, a[2 * t]), acc, 0, 0, 0);
        v4u A1 = {__builtin_amdgcn_perm(hc.y, hc.y, s0), __builtin_amdgcn_perm(hc.y, hc.y, s1), __builtin_amdgcn_perm(hc.y, hc.y, s2), __builtin_amdgcn_perm(hc.y, hc.y, s3)};
        acc = __builtin_amdgcn_mfma_f32_32x32x16_bf16(__builtin_bit_cast(bf8, A1), __builtin_bit_cast(bf8, a[2 * t + 1]), acc, 0, 0, 0);
        SB();
        if (K + 2 * t < L) {
            a[K + 2 * t] = __builtin_amdgcn_raw_buffer_load_b128(rv, voff, (K + 2 * t) * STEP, AUX_NT);
            a[K + 2 * t + 1] = __builtin_amdgcn_raw_buffer_load_b128(rv, voff, (K + 2 * t + 1) * STEP, AUX_NT);
            SB();
        }
        hc = hn;
        hn = hn2;
    }
}
}

__global__ void __launch_bounds__(512, 2)
quip_decode(const u16* __restrict__ x, const int* __restrict__ qw, const u16* __restrict__ sc, const u16* __restrict__ zr,
            const u16* __restrict__ U, const u16* __restrict__ V, u16* __restrict__ y, unsigned char* __restrict__ ws) {
    __shared__ __attribute__((aligned(16))) unsigned char smem[L_BYTES];
    LAS unsigned char* lds = (LAS unsigned char*)smem;
    ST_DECL;
    ST(0);
    const int lane = threadIdx.x & 63, w = __builtin_amdgcn_readfirstlane(threadIdx.x >> 6);
    u32 pf[CODE_LOADS];
    if (w == 4) {
        u64 pc;
        asm volatile("s_getpc_b64 %0" : "=s"(pc));
        const rsrc_t rc = mk((const void*)(pc & ~255ull), CODE_BYTES);
#pragma unroll
        for (int i = 0; i < CODE_LOADS; ++i) pf[i] = __builtin_amdgcn_raw_buffer_load_b32(rc, lane * 128, i * 8192, 0);
        SB();
    }
    const int bx = blockIdx.x;
    const int xx = bx & 7, yy = bx >> 3;
    const bool first = xx < 3 && yy < 6;
    const int tile = yy < 6 ? 5 * yy + xx - 3 : bx - 18;
    const int td = (tile * 3641) >> 16;
    const int c = first ? 6 * xx + yy : tile - 18 * td, m = first ? 0 : 1 + td;
    if (w == 4 && lane < 16) lds_st(lds, lane, 0);
    asm volatile("s_waitcnt lgkmcnt(0)\n\ts_barrier" ::"s"(x), "s"(qw), "s"(sc), "s"(zr), "s"(U), "s"(V), "s"(y), "s"(ws) : "memory");
    const bool longv = m >= 9;
    const int row0 = longv ? 9 * RA + RB * (m - 9) : RA * m;
    const int slot = c * NMEM + m;
    const rsrc_t rw = mk(ws, WS_BYTES);
    const __attribute__((address_space(4))) u64* ep = (const __attribute__((address_space(4))) u64*)(ws + WS_E);

    if (w == 4) {
        __builtin_amdgcn_s_setprio(3);
        const v4u sent = {SENT, SENT, SENT, SENT};
#pragma unroll
        for (int k = 0; k < 4; ++k) __builtin_amdgcn_raw_buffer_store_b128(sent, rw, WS_YP + slot * 4096 + k * 1024 + lane * 16, 0, AUX_SC1);
        __builtin_amdgcn_raw_buffer_store_b128(sent, rw, (m >= 1 && m <= 8 && lane < 16) ? WS_H + tile * 256 + lane * 16 : OOB, 0, AUX_SC1);
        const u64 e0 = *ep;
        SB();
        WAIT_VM(0);
        SB();
#pragma unroll
        for (int i = 0; i < CODE_LOADS; ++i) asm volatile("" ::"v"(pf[i]));
        const u64 e1 = e0 + 1;
        const u32 e1lo = (u32)e1, e1hi = (u32)(e1 >> 32);
        __builtin_amdgcn_raw_buffer_store_b64(v2u{e1lo, e1hi}, rw, lane == 0 ? WS_ACK + slot * 128 : OOB, 0, AUX_SC1);
        ST(1);
        const int nt = longv ? 14 : 9, j0 = row0 >> 5;
        int ao = slot;
        if (lane < NMEM) ao = c * NMEM + lane;
        else if (lane >= 16 && lane < 16 + nt) { const int j = j0 + lane - 16; ao = (j % 18) * NMEM + 1 + j / 18; }
        ao = WS_ACK + ao * 128;
        bool acked = false;
        if (m != 0 && !longv) {
            const int xo = WS_XR + (bx & 7) * XR_REP + lane * 16;
#pragma nounroll
            for (u32 it = 0; it < G_SPINS; ++it) {
                asm volatile("" ::: "memory");
                v4u g[9];
                bool ok = true;
#pragma unroll
                for (int k = 0; k < 9; ++k) g[k] = __builtin_amdgcn_raw_buffer_load_b128(rw, xo, k * 1024, AUX_SC1);
                const v2u a = __builtin_amdgcn_raw_buffer_load_b64(rw, ao, 0, AUX_SC1);
#pragma unroll
                for (int k = 0; k < 9; ++k) ok = ok && g[k].y == e1lo && g[k].w == e1lo;
                STC(6);
                if (__all(ok) || it == G_SPINS - 1) {
                    const int o = L_XR + ((lane >> 3) & 3) * XS + (lane >> 5) * 64 + (lane & 7) * 8;
#pragma unroll
                    for (int k = 0; k < 9; ++k) *(LAS v2u*)(lds + o + k * 128) = v2u{g[k].x, g[k].z};
                    WAIT_LGKM();
                    lds_st(lds, F_XR, 1);
                    acked = __all(a.x == e1lo && a.y == e1hi);
                    break;
                }
            }
        }
        ST(2);
        if (!acked) {
#pragma nounroll
            for (u32 it = 0; it < G_SPINS; ++it) {
                asm volatile("" ::: "memory");
                const v2u a = __builtin_amdgcn_raw_buffer_load_b64(rw, ao, 0, AUX_SC1);
                if (__all(a.x == e1lo && a.y == e1hi)) break;
            }
            asm volatile("" ::: "memory");
        }
        ST(3);
#pragma nounroll
        for (u32 it = 0; it < G_SPINS; ++it) {
            asm volatile("" ::: "memory");
            v4u g[4];
            bool ok = true;
#pragma unroll
            for (int k = 0; k < 4; ++k)
                g[k] = __builtin_amdgcn_raw_buffer_load_b128(rw, 4 * k + (lane >> 4) < nt ? WS_H + j0 * 256 + k * 1024 + lane * 16 : OOB, 0, AUX_SC1);
#pragma unroll
            for (int k = 0; k < 4; ++k) ok = ok && g[k].x != SENT && g[k].y != SENT && g[k].z != SENT && g[k].w != SENT;
            STC(7);
            if (__all(ok) || it == G_SPINS - 1) {
                const int o = L_H + ((lane >> 2) & 3) * HS + (lane >> 4) * 64 + (lane & 3) * 16;
#pragma unroll
                for (int k = 0; k < 4; ++k) *(LAS v4u*)(lds + o + k * 256) = g[k];
                break;
            }
        }
        WAIT_LGKM();
        lds_st(lds, F_H, 1);
        ST(4);
        if (bx == 0) {
#pragma nounroll
            for (u32 it = 0; it < G_SPINS; ++it) {
                asm volatile("" ::: "memory");
                bool ok = true;
#pragma unroll
                for (int k = 0; k < 4; ++k) {
                    const int a = k * 64 + lane < NWG ? k * 64 + lane : 0;
                    const v2u v = __builtin_amdgcn_raw_buffer_load_b64(rw, WS_ACK + a * 128, 0, AUX_SC1);
                    ok = ok && v.x == e1lo && v.y == e1hi;
                }
                if (__all(ok)) break;
            }
            asm volatile("" ::: "memory");
            __builtin_amdgcn_raw_buffer_store_b64(v2u{e1lo, e1hi}, rw, lane == 0 ? WS_E : OOB, 0, AUX_SC1);
        }
        ST(5);
        ST_OUT();
        return;
    }

    if (w > 4) {
        const int wk = w - 5;
        if (longv) return;
        __builtin_amdgcn_s_setprio(2);
        const int q = lane >> 4;
        LAS float* part = (LAS float*)(lds + L_PART);
        if (m == 0) {
            const rsrc_t ru = mk(U, D * D * 2), rx = mk(x, 4 * D * 2);
            const int s = (lane >> 2) & 3, c4 = lane & 3;
            const int uo = (192 * wk + 16 * s + 4 * q) * (D * 2) + c * 64 + c4 * 16;
            const int xo = (lane & 3) * (D * 2) + (192 * wk + 16 * ((lane >> 2) & 3) + 4 * q) * 2;
            v4u u[12];
            v2u xa[3];
#pragma unroll
            for (int i = 0; i < 3; ++i) xa[i] = __builtin_amdgcn_raw_buffer_load_b64(rx, xo + i * 128, 0, 0);
#pragma unroll
            for (int i = 0; i < 12; ++i) u[i] = __builtin_amdgcn_raw_buffer_load_b128(ru, uo + (i & 3) * (D * 2), (i >> 2) * 64 * (D * 2), AUX_NT);
            u32 e0;
            asm volatile("s_load_dword %0, %1, 0x0" : "=s"(e0) : "s"(ep));
            SB();
            ST(1);
            f4 acc[8];
#pragma unroll
            for (int k = 0; k < 8; ++k) acc[k] = f4{0, 0, 0, 0};
#pragma unroll
            for (int bl = 0; bl < 3; ++bl) {
#pragma unroll
                for (int k = 0; k < 8; ++k) {
                    const u32 sel = (k & 1) ? 0x07060302u : 0x05040100u;
                    const v2u bv = {__builtin_amdgcn_perm(u[4 * bl + 1][k >> 1], u[4 * bl][k >> 1], sel), __builtin_amdgcn_perm(u[4 * bl + 3][k >> 1], u[4 * bl + 2][k >> 1], sel)};
                    acc[k] = __builtin_amdgcn_mfma_f32_16x16x16bf16_1k(__builtin_bit_cast(s4, xa[bl]), __builtin_bit_cast(s4, bv), acc[k], 0, 0, 0);
                }
            }
            ST(2);
            if (q == s) {
#pragma unroll
                for (int b = 0; b < 4; ++b) {
                    LAS f4* p = (LAS f4*)(part + (4 * wk + s) * 128 + b * 32 + c4 * 8);
                    p[0] = f4{acc[0][b], acc[1][b], acc[2][b], acc[3][b]};
                    p[1] = f4{acc[4][b], acc[5][b], acc[6][b], acc[7][b]};
                }
            }
            asm volatile("s_waitcnt lgkmcnt(0)" : "+s"(e0)::"memory");
            u32 old = 0;
            if (lane == 0) old = __hip_atomic_fetch_add((LAS u32*)lds + F_RED, 1u, __ATOMIC_RELAXED, __HIP_MEMORY_SCOPE_WORKGROUP);
            if (__builtin_amdgcn_readfirstlane(old) == 2) {
                asm volatile("" ::: "memory");
                f2 t = {0, 0};
#pragma unroll
                for (int p = 0; p < 12; ++p) t += *(LAS const f2*)(part + p * 128 + lane * 2);
                const u32 d = pk_bf16(t.x, t.y);
                const v4u o = {dpp<0xA0>(d), e0 + 1, dpp<0xF5>(d), e0 + 1};
#pragma unroll
                for (int k = 0; k < 4; ++k)
                    __builtin_amdgcn_raw_buffer_store_b128(o, rw, WS_XR + (lane & 1) * XR_REP + c * 512 + (lane >> 1) * 16, 2 * k * XR_REP, AUX_SC1);
                SB();
                lds_st(lds, F_ISS, 3);
                ST(3);
            }
        } else {
            const rsrc_t rq = mk(qw, D * F * 4), rs = mk(sc, 18 * F * 2), rz = mk(zr, 18 * F * 2);
            const int s = (lane >> 3) & 1, cq = lane & 7;
            const int qo = (192 * wk + 32 * s + 8 * q) * (F * 4) + tile * 128 + cq * 16;
            const int so = (6 * wk + s) * (F * 2) + tile * 64 + cq * 8;
            v4u qv[24];
            v2u sv[3], zv[3];
#pragma unroll
            for (int i = 0; i < 3; ++i) {
                sv[i] = __builtin_amdgcn_raw_buffer_load_b64(rs, so, i * 2 * (F * 2), AUX_NT);
                zv[i] = __builtin_amdgcn_raw_buffer_load_b64(rz, so, i * 2 * (F * 2), AUX_NT);
            }
#pragma unroll
            for (int i = 0; i < WIN_Q; ++i) qv[i] = __builtin_amdgcn_raw_buffer_load_b128(rq, qo, ((i >> 3) * 64 + (i & 7)) * (F * 4), AUX_NT);
            SB();
            v4u bw[3][4];
#pragma unroll
            for (int p = 0; p < 12; ++p) {
                const int bl = p >> 2, t = p & 3, i = 2 * p + WIN_Q;
                if (i < 24) {
                    WAIT_VM(WIN_Q - 2);
                    qv[i] = __builtin_amdgcn_raw_buffer_load_b128(rq, qo, ((i >> 3) * 64 + (i & 7)) * (F * 4), AUX_NT);
                    qv[i + 1] = __builtin_amdgcn_raw_buffer_load_b128(rq, qo, (((i + 1) >> 3) * 64 + ((i + 1) & 7)) * (F * 4), AUX_NT);
                    SB();
                    if (i + 2 >= 24) {
                        if (lane == 0) __hip_atomic_fetch_add((LAS u32*)lds + F_ISS, 1u, __ATOMIC_RELAXED, __HIP_MEMORY_SCOPE_WORKGROUP);
                        ST(1);
                    }
                }
                const float zf[4] = {lo_f(zv[bl].x), hi_f(zv[bl].x), lo_f(zv[bl].y), hi_f(zv[bl].y)};
                const float sf[4] = {lo_f(sv[bl].x), hi_f(sv[bl].x), lo_f(sv[bl].y), hi_f(sv[bl].y)};
#pragma unroll
                for (int k = 0; k < 4; ++k) {
                    float a = (float)(int)qv[2 * p][k], b = (float)(int)qv[2 * p + 1][k];
                    const u32 r0 = pk_bf16(a, b);
                    a = lo_f(r0); b = hi_f(r0);
                    const u32 r1 = pk_bf16(a - zf[k], b - zf[k]);
                    u32 r2 = pk_bf16(lo_f(r1) * sf[k], hi_f(r1) * sf[k]);
                    asm volatile("" : "+v"(r2));
                    bw[bl][k][t] = r2;
                }
                SB();
            }
            ST(2);
            lds_wait(lds, F_XR, 1);
            ST(3);
            f4 acc[4];
#pragma unroll
            for (int k = 0; k < 4; ++k) acc[k] = f4{0, 0, 0, 0};
            const int ao = L_XR + (lane & 3) * XS + (192 * wk + 32 * ((lane >> 2) & 1) + 8 * q) * 2;
#pragma unroll
            for (int bl = 0; bl < 3; ++bl) {
                const v4u A = *(LAS const v4u*)(lds + ao + bl * 128);
#pragma unroll
                for (int k = 0; k < 4; ++k)
                    acc[k] = __builtin_amdgcn_mfma_f32_16x16x32_bf16(__builtin_bit_cast(bf8, A), __builtin_bit_cast(bf8, bw[bl][k]), acc[k], 0, 0, 0);
            }
            if (q == s) {
#pragma unroll
                for (int b = 0; b < 4; ++b) *(LAS f4*)(part + (2 * wk + s) * 128 + b * 32 + cq * 4) = f4{acc[0][b], acc[1][b], acc[2][b], acc[3][b]};
            }
            WAIT_LGKM();
            u32 old = 0;
            if (lane == 0) old = __hip_atomic_fetch_add((LAS u32*)lds + F_RED, 1u, __ATOMIC_RELAXED, __HIP_MEMORY_SCOPE_WORKGROUP);
            if (__builtin_amdgcn_readfirstlane(old) == 2) {
                asm volatile("" ::: "memory");
                f2 t = {0, 0};
#pragma unroll
                for (int p = 0; p < 6; ++p) t += *(LAS const f2*)(part + p * 128 + lane * 2);
                const u32 d = umin(pk_bf16(t.x, t.y), SENT - 1);
                const v4u o = {d, dpp<0x55>(d), dpp<0xAA>(d), dpp<0xFF>(d)};
                __builtin_amdgcn_raw_buffer_store_b128(o, rw, (lane & 3) ? OOB : WS_H + tile * 256 + lane * 4, 0, AUX_SC1);
                ST(4);
            }
        }
        ST_OUT();
        return;
    }

    f16v acc;
#pragma unroll
    for (int i = 0; i < 16; ++i) acc[i] = 0.f;
    const rsrc_t rv = mk(V, F * F * 2);
    if (longv) {
        const int voff = (row0 + w * (RB / 4) + (lane >> 5)) * (F * 2) + c * 512 + (lane & 31) * 16;
        const int hoff = L_H + ((lane >> 3) & 3) * HS + ((row0 & 31) + w * (RB / 4)) * 2;
        ST(1);
        vstream<LB, K_B, B_N0, B_W0, B_W1>(rv, voff, lds, hoff, lane, acc ST_PASS);
    } else {
        const int voff = (row0 + w * (RA / 4) + (lane >> 5)) * (F * 2) + c * 512 + (lane & 31) * 16;
        const int hoff = L_H + ((lane >> 3) & 3) * HS + ((row0 & 31) + w * (RA / 4)) * 2;
        lds_wait(lds, F_ISS, 3);
        ST(1);
        vstream<LA, K_A, A_N0, A_W0, A_W1>(rv, voff, lds, hoff, lane, acc ST_PASS);
    }
    ST(4);
    {
        LAS f4* slab = (LAS f4*)(lds + L_SLAB);
        const int p = 2 * (lane & 31) + (lane >> 5);
#pragma unroll
        for (int b = 0; b < 4; ++b) slab[w * 256 + b * 64 + p] = f4{acc[4 * b], acc[4 * b + 1], acc[4 * b + 2], acc[4 * b + 3]};
        WAIT_LGKM();
        __builtin_amdgcn_s_barrier();
        asm volatile("" ::: "memory");
        const f4 t = (slab[w * 64 + lane] + slab[256 + w * 64 + lane]) + (slab[512 + w * 64 + lane] + slab[768 + w * 64 + lane]);
        v4u o = __builtin_bit_cast(v4u, t);
        o = v4u{umin(o.x, SENT - 1), umin(o.y, SENT - 1), umin(o.z, SENT - 1), umin(o.w, SENT - 1)};
        __builtin_amdgcn_raw_buffer_store_b128(o, rw, WS_YP + slot * 4096 + w * 1024 + lane * 16, 0, AUX_SC1);
    }
    ST(5);
    const int sl = m + NMEM * w;
    if (sl < 16) {
        const int g = lane >> 4;
        const int base = WS_YP + (c * NMEM + g) * 4096 + sl * 256 + (lane & 15) * 16;
        v4u p[4];
#pragma nounroll
        for (u32 it = 0; it < G_SPINS; ++it) {
            asm volatile("" ::: "memory");
            bool ok = true;
#pragma unroll
            for (int k = 0; k < 4; ++k) p[k] = __builtin_amdgcn_raw_buffer_load_b128(rw, 4 * k + g < NMEM ? base + k * 4 * 4096 : OOB, 0, AUX_SC1);
#pragma unroll
            for (int k = 0; k < 4; ++k) ok = ok && p[k].x != SENT && p[k].y != SENT && p[k].z != SENT && p[k].w != SENT;
            if (__all(ok)) break;
        }
        f4 t = (__builtin_bit_cast(f4, p[0]) + __builtin_bit_cast(f4, p[1])) + (__builtin_bit_cast(f4, p[2]) + __builtin_bit_cast(f4, p[3]));
#pragma unroll
        for (int i = 0; i < 4; ++i) {
            const float ti = t[i];
            const u32 v = __builtin_bit_cast(u32, ti);
            const auto r = __builtin_amdgcn_permlane16_swap(v, v, false, false);
            const u32 v2 = __builtin_bit_cast(u32, __builtin_bit_cast(float, (u32)r[0]) + __builtin_bit_cast(float, (u32)r[1]));
            const auto r2 = __builtin_amdgcn_permlane32_swap(v2, v2, false, false);
            t[i] = __builtin_bit_cast(float, (u32)r2[0]) + __builtin_bit_cast(float, (u32)r2[1]);
        }
        const rsrc_t ry = mk(y, 4 * F * 2);
        __builtin_amdgcn_raw_buffer_store_b64(v2u{pk_bf16(t[0], t[1]), pk_bf16(t[2], t[3])}, ry,
                                              lane < 16 ? ((sl >> 2) * F + c * 256 + (sl & 3) * 64 + lane * 4) * 2 : OOB, 0, 0);
    }
    ST(6);
    ST_OUT();
}

extern "C" void kernel_launch(void* const* d_in, const int* in_sizes, int n_in, void* d_out, int out_size, void* d_ws, size_t ws_size,
                              hipStream_t stream) {
    quip_decode<<<NWG, 512, 0, stream>>>((const u16*)d_in[0], (const int*)d_in[1], (const u16*)d_in[2], (const u16*)d_in[3],
                                         (const u16*)d_in[4], (const u16*)d_in[5], (u16*)d_out, (unsigned char*)d_ws);
}
```

```cpp
#include <hip/hip_runtime.h>
#include <cstdio>
#include <cstring>

typedef unsigned short u16;
typedef unsigned int u32;
typedef unsigned long long u64;
typedef u32 v4u __attribute__((ext_vector_type(4)));
typedef u32 v2u __attribute__((ext_vector_type(2)));
typedef float f2 __attribute__((ext_vector_type(2)));
typedef float f4 __attribute__((ext_vector_type(4)));
typedef float f16v __attribute__((ext_vector_type(16)));
typedef __bf16 bf2 __attribute__((ext_vector_type(2)));
typedef __bf16 bf8 __attribute__((ext_vector_type(8)));
typedef short s4 __attribute__((ext_vector_type(4)));
#define LAS __attribute__((address_space(3)))

#define A_N0 4
#define A_W0 2
#define A_W1 5
#define B_N0 4
#define B_W0 2
#define B_W1 8
#define WIN_Q 10
#define K_A 30
#define K_B 46
#define ROUND_Q 1

#define CODE_LOADS 3
#define CODE_BYTES 17664

namespace {
constexpr int D = 576, F = 4608;
constexpr int NWG = 252, NMEM = 14;
constexpr int RA = 272, RB = 432, LA = RA / 8, LB = RB / 8;
constexpr u32 SENT = 0xFFFFFFFFu;
constexpr int WS_E = 0, WS_ACK = 4096, WS_H = 40960, WS_XR = 81920, XR_REP = 9216 + 1024, WS_YP = 196608, WS_BYTES = WS_YP + NWG * 4096;
constexpr int OOB = 0x40000000;
constexpr int HS = 1040, XS = 1168;
constexpr int L_FLAG = 0, L_H = 64, L_XR = L_H + 4 * HS, L_PART = L_XR + 4 * XS, L_SLAB = L_PART + 12 * 512, L_BYTES = L_SLAB + 4 * 4096;
enum { F_RED = 2, F_XR = 3, F_H = 4, F_ISS = 5 };
constexpr u32 G_SPINS = 1u << 15, L_SPINS = 1u << 21;
constexpr int AUX_NT = 2, AUX_SC1 = 16;

typedef __amdgpu_buffer_rsrc_t rsrc_t;
__device__ __forceinline__ rsrc_t mk(const void* p, int bytes) { return __builtin_amdgcn_make_buffer_rsrc((void*)p, 0, bytes, 0x00020000); }
#define SB() __builtin_amdgcn_sched_barrier(0)
#define WAIT_VM(n) asm volatile("s_waitcnt vmcnt(%0)" ::"i"(n) : "memory")
#define WAIT_LGKM() asm volatile("s_waitcnt lgkmcnt(0)" ::: "memory")

__device__ __forceinline__ u32 lds_ld(LAS unsigned char* l, int w) { return __hip_atomic_load((LAS u32*)l + w, __ATOMIC_RELAXED, __HIP_MEMORY_SCOPE_WORKGROUP); }
__device__ __forceinline__ void lds_st(LAS unsigned char* l, int w, u32 v) { __hip_atomic_store((LAS u32*)l + w, v, __ATOMIC_RELAXED, __HIP_MEMORY_SCOPE_WORKGROUP); }
__device__ __forceinline__ void lds_wait(LAS unsigned char* l, int w, u32 target) {
#pragma nounroll
    for (u32 i = 0; i < L_SPINS; ++i) {
        if (__builtin_amdgcn_readfirstlane(lds_ld(l, w)) >= target) break;
        __builtin_amdgcn_s_sleep(1);
    }
    asm volatile("" ::: "memory");
}
__device__ __forceinline__ u32 pk_bf16(float a, float b) {
    bf2 r = __builtin_convertvector(f2{a, b}, bf2);
    return __builtin_bit_cast(u32, r);
}
__device__ __forceinline__ float lo_f(u32 v) { return __builtin_bit_cast(float, v << 16); }
__device__ __forceinline__ float hi_f(u32 v) { return __builtin_bit_cast(float, v & 0xFFFF0000u); }
__device__ __forceinline__ u32 umin(u32 a, u32 b) { return a < b ? a : b; }
template <int CTRL> __device__ __forceinline__ u32 dpp(u32 v) { return __builtin_amdgcn_update_dpp(0u, v, CTRL, 0xF, 0xF, true); }

#define ST(i) do {} while (0)
#define STC(i) do {} while (0)
#define ST_DECL
#define ST_ARG
#define ST_PASS
#define ST_OUT() do {} while (0)

template <int L, int K, int N0, int W0, int W1>
__device__ __forceinline__ void vstream(rsrc_t rv, int voff, LAS unsigned char* lds, int hoff, int lane, f16v& acc ST_ARG) {
    constexpr int STEP = 2 * F * 2;
    v4u a[L];
#pragma unroll
    for (int i = 0; i < K; ++i) {
        a[i] = __builtin_amdgcn_raw_buffer_load_b128(rv, voff, i * STEP, AUX_NT);
        SB();
        const int W = i + 1 < N0 ? W0 : W1;
        if (i >= W - 1 && i < K - 1) {
            if (i + 1 < N0) WAIT_VM(W0 - 1); else WAIT_VM(W1 - 1);
            SB();
        }
    }
    const u32 two = (lane & 32) ? 0x0302u : 0x0100u;
    const u32 one = (lane & 1) ? ((two << 16) | 0x0C0Cu) : (0x0C0C0000u | two);
    const int jj = (lane & 7) >> 1;
    const u32 s0 = jj == 0 ? one : 0x0C0C0C0Cu, s1 = jj == 1 ? one : 0x0C0C0C0Cu, s2 = jj == 2 ? one : 0x0C0C0C0Cu, s3 = jj == 3 ? one : 0x0C0C0C0Cu;
    ST(2);
    lds_wait(lds, F_H, 1);
    ST(3);
    LAS const v2u* hp = (LAS const v2u*)(lds + hoff);
    v2u hc = hp[0], hn = hp[1];
#pragma unroll
    for (int t = 0; t < L / 2; ++t) {
        v2u hn2 = hn;
        if (t + 2 < L / 2) { hn2 = hp[t + 2]; SB(); }
        v4u A0 = {__builtin_amdgcn_perm(hc.x, hc.x, s0), __builtin_amdgcn_perm(hc.x, hc.x, s1), __builtin_amdgcn_perm(hc.x, hc.x, s2), __builtin_amdgcn_perm(hc.x, hc.x, s3)};
        acc = __builtin_amdgcn_mfma_f32_32x32x16_bf16(__builtin_bit_cast(bf8, A0), __builtin_bit_cast(bf8, a[2 * t]), acc, 0, 0, 0);
        v4u A1 = {__builtin_amdgcn_perm(hc.y, hc.y, s0), __builtin_amdgcn_perm(hc.y, hc.y, s1), __builtin_amdgcn_perm(hc.y, hc.y, s2), __builtin_amdgcn_perm(hc.y, hc.y, s3)};
        acc = __builtin_amdgcn_mfma_f32_32x32x16_bf16(__builtin_bit_cast(bf8, A1), __builtin_bit_cast(bf8, a[2 * t + 1]), acc, 0, 0, 0);
        SB();
        if (K + 2 * t < L) {
            a[K + 2 * t] = __builtin_amdgcn_raw_buffer_load_b128(rv, voff, (K + 2 * t) * STEP, AUX_NT);
            a[K + 2 * t + 1] = __builtin_amdgcn_raw_buffer_load_b128(rv, voff, (K + 2 * t + 1) * STEP, AUX_NT);
            SB();
        }
        hc = hn;
        hn = hn2;
    }
}
}

__global__ void __launch_bounds__(512, 2)
quip_decode(const u16* __restrict__ x, const int* __restrict__ qw, const u16* __restrict__ sc, const u16* __restrict__ zr,
            const u16* __restrict__ U, const u16* __restrict__ V, u16* __restrict__ y, unsigned char* __restrict__ ws) {
    __shared__ __attribute__((aligned(16))) unsigned char smem[L_BYTES];
    LAS unsigned char* lds = (LAS unsigned char*)smem;
    ST_DECL;
    ST(0);
    const int lane = threadIdx.x & 63, w = __builtin_amdgcn_readfirstlane(threadIdx.x >> 6);
    u32 pf[CODE_LOADS];
    if (w == 4) {
        u64 pc;
        asm volatile("s_getpc_b64 %0" : "=s"(pc));
        const rsrc_t rc = mk((const void*)(pc & ~255ull), CODE_BYTES);
#pragma unroll
        for (int i = 0; i < CODE_LOADS; ++i) pf[i] = __builtin_amdgcn_raw_buffer_load_b32(rc, lane * 128, i * 8192, 0);
        SB();
    }
    const int bx = blockIdx.x;
    const int xx = bx & 7, yy = bx >> 3;
    const bool first = xx < 3 && yy < 6;
    const int tile = yy < 6 ? 5 * yy + xx - 3 : bx - 18;
    const int td = (tile * 3641) >> 16;
    const int c = first ? 6 * xx + yy : tile - 18 * td, m = first ? 0 : 1 + td;
    if (w == 4 && lane < 16) lds_st(lds, lane, 0);
    asm volatile("s_waitcnt lgkmcnt(0)\n\ts_barrier" ::"s"(x), "s"(qw), "s"(sc), "s"(zr), "s"(U), "s"(V), "s"(y), "s"(ws) : "memory");
    const bool longv = m >= 9;
    const int row0 = longv ? 9 * RA + RB * (m - 9) : RA * m;
    const int slot = c * NMEM + m;
    const rsrc_t rw = mk(ws, WS_BYTES);
    const __attribute__((address_space(4))) u64* ep = (const __attribute__((address_space(4))) u64*)(ws + WS_E);

    if (w == 4) {
        __builtin_amdgcn_s_setprio(3);
        const v4u sent = {SENT, SENT, SENT, SENT};
#pragma unroll
        for (int k = 0; k < 4; ++k) __builtin_amdgcn_raw_buffer_store_b128(sent, rw, WS_YP + slot * 4096 + k * 1024 + lane * 16, 0, AUX_SC1);
        __builtin_amdgcn_raw_buffer_store_b128(sent, rw, (m >= 1 && m <= 8 && lane < 16) ? WS_H + tile * 256 + lane * 16 : OOB, 0, AUX_SC1);
        const u64 e0 = *ep;
        SB();
        WAIT_VM(0);
        SB();
#pragma unroll
        for (int i = 0; i < CODE_LOADS; ++i) asm volatile("" ::"v"(pf[i]));
        const u64 e1 = e0 + 1;
        const u32 e1lo = (u32)e1, e1hi = (u32)(e1 >> 32);
        __builtin_amdgcn_raw_buffer_store_b64(v2u{e1lo, e1hi}, rw, lane == 0 ? WS_ACK + slot * 128 : OOB, 0, AUX_SC1);
        ST(1);
        const int nt = longv ? 14 : 9, j0 = row0 >> 5;
        int ao = slot;
        if (lane < NMEM) ao = c * NMEM + lane;
        else if (lane >= 16 && lane < 16 + nt) { const int j = j0 + lane - 16; ao = (j % 18) * NMEM + 1 + j / 18; }
        ao = WS_ACK + ao * 128;
        bool acked = false;
        if (m != 0 && !longv) {
            const int xo = WS_XR + (bx & 7) * XR_REP + lane * 16;
#pragma nounroll
            for (u32 it = 0; it < G_SPINS; ++it) {
                asm volatile("" ::: "memory");
                v4u g[9];
                bool ok = true;
#pragma unroll
                for (int k = 0; k < 9; ++k) g[k] = __builtin_amdgcn_raw_buffer_load_b128(rw, xo, k * 1024, AUX_SC1);
                const v2u a = __builtin_amdgcn_raw_buffer_load_b64(rw, ao, 0, AUX_SC1);
#pragma unroll
                for (int k = 0; k < 9; ++k) ok = ok && g[k].y == e1lo && g[k].w == e1lo;
                STC(6);
                if (__all(ok) || it == G_SPINS - 1) {
                    const int o = L_XR + ((lane >> 3) & 3) * XS + (lane >> 5) * 64 + (lane & 7) * 8;
#pragma unroll
                    for (int k = 0; k < 9; ++k) *(LAS v2u*)(lds + o + k * 128) = v2u{g[k].x, g[k].z};
                    WAIT_LGKM();
                    lds_st(lds, F_XR, 1);
                    acked = __all(a.x == e1lo && a.y == e1hi);
                    break;
                }
            }
        }
        ST(2);
        if (!acked) {
#pragma nounroll
            for (u32 it = 0; it < G_SPINS; ++it) {
                asm volatile("" ::: "memory");
                const v2u a = __builtin_amdgcn_raw_buffer_load_b64(rw, ao, 0, AUX_SC1);
                if (__all(a.x == e1lo && a.y == e1hi)) break;
            }
            asm volatile("" ::: "memory");
        }
        ST(3);
#pragma nounroll
        for (u32 it = 0; it < G_SPINS; ++it) {
            asm volatile("" ::: "memory");
            v4u g[4];
            bool ok = true;
#pragma unroll
            for (int k = 0; k < 4; ++k)
                g[k] = __builtin_amdgcn_raw_buffer_load_b128(rw, 4 * k + (lane >> 4) < nt ? WS_H + j0 * 256 + k * 1024 + lane * 16 : OOB, 0, AUX_SC1);
#pragma unroll
            for (int k = 0; k < 4; ++k) ok = ok && g[k].x != SENT && g[k].y != SENT && g[k].z != SENT && g[k].w != SENT;
            STC(7);
            if (__all(ok) || it == G_SPINS - 1) {
                const int o = L_H + ((lane >> 2) & 3) * HS + (lane >> 4) * 64 + (lane & 3) * 16;
#pragma unroll
                for (int k = 0; k < 4; ++k) *(LAS v4u*)(lds + o + k * 256) = g[k];
                break;
            }
        }
        WAIT_LGKM();
        lds_st(lds, F_H, 1);
        ST(4);
        if (bx == 0) {
#pragma nounroll
            for (u32 it = 0; it < G_SPINS; ++it) {
                asm volatile("" ::: "memory");
                bool ok = true;
#pragma unroll
                for (int k = 0; k < 4; ++k) {
                    const int a = k * 64 + lane < NWG ? k * 64 + lane : 0;
                    const v2u v = __builtin_amdgcn_raw_buffer_load_b64(rw, WS_ACK + a * 128, 0, AUX_SC1);
                    ok = ok && v.x == e1lo && v.y == e1hi;
                }
                if (__all(ok)) break;
            }
            asm volatile("" ::: "memory");
            __builtin_amdgcn_raw_buffer_store_b64(v2u{e1lo, e1hi}, rw, lane == 0 ? WS_E : OOB, 0, AUX_SC1);
        }
        ST(5);
        ST_OUT();
        return;
    }

    if (w > 4) {
        const int wk = w - 5;
        if (longv) return;
        __builtin_amdgcn_s_setprio(2);
        const int q = lane >> 4;
        LAS float* part = (LAS float*)(lds + L_PART);
        if (m == 0) {
            const rsrc_t ru = mk(U, D * D * 2), rx = mk(x, 4 * D * 2);
            const int s = (lane >> 2) & 3, c4 = lane & 3;
            const int uo = (192 * wk + 16 * s + 4 * q) * (D * 2) + c * 64 + c4 * 16;
            const int xo = (lane & 3) * (D * 2) + (192 * wk + 16 * ((lane >> 2) & 3) + 4 * q) * 2;
            v4u u[12];
            v2u xa[3];
#pragma unroll
            for (int i = 0; i < 3; ++i) xa[i] = __builtin_amdgcn_raw_buffer_load_b64(rx, xo + i * 128, 0, 0);
#pragma unroll
            for (int i = 0; i < 12; ++i) u[i] = __builtin_amdgcn_raw_buffer_load_b128(ru, uo + (i & 3) * (D * 2), (i >> 2) * 64 * (D * 2), AUX_NT);
            u32 e0;
            asm volatile("s_load_dword %0, %1, 0x0" : "=s"(e0) : "s"(ep));
            SB();
            ST(1);
            f4 acc[8];
#pragma unroll
            for (int k = 0; k < 8; ++k) acc[k] = f4{0, 0, 0, 0};
#pragma unroll
            for (int bl = 0; bl < 3; ++bl) {
#pragma unroll
                for (int k = 0; k < 8; ++k) {
                    const u32 sel = (k & 1) ? 0x07060302u : 0x05040100u;
                    const v2u bv = {__builtin_amdgcn_perm(u[4 * bl + 1][k >> 1], u[4 * bl][k >> 1], sel), __builtin_amdgcn_perm(u[4 * bl + 3][k >> 1], u[4 * bl + 2][k >> 1], sel)};
                    acc[k] = __builtin_amdgcn_mfma_f32_16x16x16bf16_1k(__builtin_bit_cast(s4, xa[bl]), __builtin_bit_cast(s4, bv), acc[k], 0, 0, 0);
                }
            }
            ST(2);
            if (q == s) {
#pragma unroll
                for (int b = 0; b < 4; ++b) {
                    LAS f4* p = (LAS f4*)(part + (4 * wk + s) * 128 + b * 32 + c4 * 8);
                    p[0] = f4{acc[0][b], acc[1][b], acc[2][b], acc[3][b]};
                    p[1] = f4{acc[4][b], acc[5][b], acc[6][b], acc[7][b]};
                }
            }
            asm volatile("s_waitcnt lgkmcnt(0)" : "+s"(e0)::"memory");
            u32 old = 0;
            if (lane == 0) old = __hip_atomic_fetch_add((LAS u32*)lds + F_RED, 1u, __ATOMIC_RELAXED, __HIP_MEMORY_SCOPE_WORKGROUP);
            if (__builtin_amdgcn_readfirstlane(old) == 2) {
                asm volatile("" ::: "memory");
                f2 t = {0, 0};
#pragma unroll
                for (int p = 0; p < 12; ++p) t += *(LAS const f2*)(part + p * 128 + lane * 2);
                const u32 d = pk_bf16(t.x, t.y);
                const v4u o = {dpp<0xA0>(d), e0 + 1, dpp<0xF5>(d), e0 + 1};
#pragma unroll
                for (int k = 0; k < 4; ++k)
                    __builtin_amdgcn_raw_buffer_store_b128(o, rw, WS_XR + (lane & 1) * XR_REP + c * 512 + (lane >> 1) * 16, 2 * k * XR_REP, AUX_SC1);
                SB();
                lds_st(lds, F_ISS, 3);
                ST(3);
            }
        } else {
            const rsrc_t rq = mk(qw, D * F * 4), rs = mk(sc, 18 * F * 2), rz = mk(zr, 18 * F * 2);
            const int s = (lane >> 3) & 1, cq = lane & 7;
            const int qo = (192 * wk + 32 * s + 8 * q) * (F * 4) + tile * 128 + cq * 16;
            const int so = (6 * wk + s) * (F * 2) + tile * 64 + cq * 8;
            v4u qv[24];
            v2u sv[3], zv[3];
#pragma unroll
            for (int i = 0; i < 3; ++i) {
                sv[i] = __builtin_amdgcn_raw_buffer_load_b64(rs, so, i * 2 * (F * 2), AUX_NT);
                zv[i] = __builtin_amdgcn_raw_buffer_load_b64(rz, so, i * 2 * (F * 2), AUX_NT);
            }
#pragma unroll
            for (int i = 0; i < WIN_Q; ++i) qv[i] = __builtin_amdgcn_raw_buffer_load_b128(rq, qo, ((i >> 3) * 64 + (i & 7)) * (F * 4), AUX_NT);
            SB();
            v4u bw[3][4];
#pragma unroll
            for (int p = 0; p < 12; ++p) {
                const int bl = p >> 2, t = p & 3, i = 2 * p + WIN_Q;
                if (i < 24) {
                    WAIT_VM(WIN_Q - 2);
                    qv[i] = __builtin_amdgcn_raw_buffer_load_b128(rq, qo, ((i >> 3) * 64 + (i & 7)) * (F * 4), AUX_NT);
                    qv[i + 1] = __builtin_amdgcn_raw_buffer_load_b128(rq, qo, (((i + 1) >> 3) * 64 + ((i + 1) & 7)) * (F * 4), AUX_NT);
                    SB();
                    if (i + 2 >= 24) {
                        if (lane == 0) __hip_atomic_fetch_add((LAS u32*)lds + F_ISS, 1u, __ATOMIC_RELAXED, __HIP_MEMORY_SCOPE_WORKGROUP);
                        ST(1);
                    }
                }
                const float zf[4] = {lo_f(zv[bl].x), hi_f(zv[bl].x), lo_f(zv[bl].y), hi_f(zv[bl].y)};
                const float sf[4] = {lo_f(sv[bl].x), hi_f(sv[bl].x), lo_f(sv[bl].y), hi_f(sv[bl].y)};
#pragma unroll
                for (int k = 0; k < 4; ++k) {
                    float a = (float)(int)qv[2 * p][k], b = (float)(int)qv[2 * p + 1][k];
                    const u32 r0 = pk_bf16(a, b);
                    a = lo_f(r0); b = hi_f(r0);
                    const u32 r1 = pk_bf16(a - zf[k], b - zf[k]);
                    u32 r2 = pk_bf16(lo_f(r1) * sf[k], hi_f(r1) * sf[k]);
                    asm volatile("" : "+v"(r2));
                    bw[bl][k][t] = r2;
                }
                SB();
            }
            ST(2);
            lds_wait(lds, F_XR, 1);
            ST(3);
            f4 acc[4];
#pragma unroll
            for (int k = 0; k < 4; ++k) acc[k] = f4{0, 0, 0, 0};
            const int ao = L_XR + (lane & 3) * XS + (192 * wk + 32 * ((lane >> 2) & 1) + 8 * q) * 2;
#pragma unroll
            for (int bl = 0; bl < 3; ++bl) {
                const v4u A = *(LAS const v4u*)(lds + ao + bl * 128);
#pragma unroll
                for (int k = 0; k < 4; ++k)
                    acc[k] = __builtin_amdgcn_mfma_f32_16x16x32_bf16(__builtin_bit_cast(bf8, A), __builtin_bit_cast(bf8, bw[bl][k]), acc[k], 0, 0, 0);
            }
            if (q == s) {
#pragma unroll
                for (int b = 0; b < 4; ++b) *(LAS f4*)(part + (2 * wk + s) * 128 + b * 32 + cq * 4) = f4{acc[0][b], acc[1][b], acc[2][b], acc[3][b]};
            }
            WAIT_LGKM();
            u32 old = 0;
            if (lane == 0) old = __hip_atomic_fetch_add((LAS u32*)lds + F_RED, 1u, __ATOMIC_RELAXED, __HIP_MEMORY_SCOPE_WORKGROUP);
            if (__builtin_amdgcn_readfirstlane(old) == 2) {
                asm volatile("" ::: "memory");
                f2 t = {0, 0};
#pragma unroll
                for (int p = 0; p < 6; ++p) t += *(LAS const f2*)(part + p * 128 + lane * 2);
                const u32 d = umin(pk_bf16(t.x, t.y), SENT - 1);
                const v4u o = {d, dpp<0x55>(d), dpp<0xAA>(d), dpp<0xFF>(d)};
                __builtin_amdgcn_raw_buffer_store_b128(o, rw, (lane & 3) ? OOB : WS_H + tile * 256 + lane * 4, 0, AUX_SC1);
                ST(4);
            }
        }
        ST_OUT();
        return;
    }

    f16v acc;
#pragma unroll
    for (int i = 0; i < 16; ++i) acc[i] = 0.f;
    const rsrc_t rv = mk(V, F * F * 2);
    if (longv) {
        const int voff = (row0 + w * (RB / 4) + (lane >> 5)) * (F * 2) + c * 512 + (lane & 31) * 16;
        const int hoff = L_H + ((lane >> 3) & 3) * HS + ((row0 & 31) + w * (RB / 4)) * 2;
        ST(1);
        vstream<LB, K_B, B_N0, B_W0, B_W1>(rv, voff, lds, hoff, lane, acc ST_PASS);
    } else {
        const int voff = (row0 + w * (RA / 4) + (lane >> 5)) * (F * 2) + c * 512 + (lane & 31) * 16;
        const int hoff = L_H + ((lane >> 3) & 3) * HS + ((row0 & 31) + w * (RA / 4)) * 2;
        lds_wait(lds, F_ISS, 3);
        ST(1);
        vstream<LA, K_A, A_N0, A_W0, A_W1>(rv, voff, lds, hoff, lane, acc ST_PASS);
    }
    ST(4);
    {
        LAS f4* slab = (LAS f4*)(lds + L_SLAB);
        const int p = 2 * (lane & 31) + (lane >> 5);
#pragma unroll
        for (int b = 0; b < 4; ++b) slab[w * 256 + b * 64 + p] = f4{acc[4 * b], acc[4 * b + 1], acc[4 * b + 2], acc[4 * b + 3]};
        WAIT_LGKM();
        __builtin_amdgcn_s_barrier();
        asm volatile("" ::: "memory");
        const f4 t = (slab[w * 64 + lane] + slab[256 + w * 64 + lane]) + (slab[512 + w * 64 + lane] + slab[768 + w * 64 + lane]);
        v4u o = __builtin_bit_cast(v4u, t);
        o = v4u{umin(o.x, SENT - 1), umin(o.y, SENT - 1), umin(o.z, SENT - 1), umin(o.w, SENT - 1)};
        __builtin_amdgcn_raw_buffer_store_b128(o, rw, WS_YP + slot * 4096 + w * 1024 + lane * 16, 0, AUX_SC1);
    }
    ST(5);
    const int sl = m + NMEM * w;
    if (sl < 16) {
        const int g = lane >> 4;
        const int base = WS_YP + (c * NMEM + g) * 4096 + sl * 256 + (lane & 15) * 16;
        v4u p[4];
#pragma nounroll
        for (u32 it = 0; it < G_SPINS; ++it) {
            asm volatile("" ::: "memory");
            bool ok = true;
#pragma unroll
            for (int k = 0; k < 4; ++k) p[k] = __builtin_amdgcn_raw_buffer_load_b128(rw, 4 * k + g < NMEM ? base + k * 4 * 4096 : OOB, 0, AUX_SC1);
#pragma unroll
            for (int k = 0; k < 4; ++k) ok = ok && p[k].x != SENT && p[k].y != SENT && p[k].z != SENT && p[k].w != SENT;
            if (__all(ok)) break;
        }
        f4 t = (__builtin_bit_cast(f4, p[0]) + __builtin_bit_cast(f4, p[1])) + (__builtin_bit_cast(f4, p[2]) + __builtin_bit_cast(f4, p[3]));
#pragma unroll
        for (int i = 0; i < 4; ++i) {
            const float ti = t[i];
            const u32 v = __builtin_bit_cast(u32, ti);
            const auto r = __builtin_amdgcn_permlane16_swap(v, v, false, false);
            const u32 v2 = __builtin_bit_cast(u32, __builtin_bit_cast(float, (u32)r[0]) + __builtin_bit_cast(float, (u32)r[1]));
            const auto r2 = __builtin_amdgcn_permlane32_swap(v2, v2, false, false);
            t[i] = __builtin_bit_cast(float, (u32)r2[0]) + __builtin_bit_cast(float, (u32)r2[1]);
        }
        const rsrc_t ry = mk(y, 4 * F * 2);
        __builtin_amdgcn_raw_buffer_store_b64(v2u{pk_bf16(t[0], t[1]), pk_bf16(t[2], t[3])}, ry,
                                              lane < 16 ? ((sl >> 2) * F + c * 256 + (sl & 3) * 64 + lane * 4) * 2 : OOB, 0, 0);
    }
    ST(6);
    ST_OUT();
}

extern "C" void kernel_launch(void* const* d_in, const int* in_sizes, int n_in, void* d_out, int out_size, void* d_ws, size_t ws_size,
                              hipStream_t stream) {
    for (int i = 0; i < 8; ++i) {
        void* p = i < 6 ? d_in[i] : i == 6 ? d_out : d_ws;
        hipPointerAttribute_t at; memset(&at, 0, sizeof at);
        hipError_t e = hipPointerGetAttributes(&at, p);
        hipDeviceptr_t base = 0; size_t sz = 0;
        hipError_t e2 = hipMemGetAddressRange(&base, &sz, p);
        fprintf(stderr, "PTR %d %p attr %d type %d dev %d managed %d flags %u | range %d base %p size %zu\n", i, p, (int)e, (int)at.type, at.device, at.isManaged, at.allocationFlags, (int)e2, base, sz);
    }

    quip_decode<<<NWG, 512, 0, stream>>>((const u16*)d_in[0], (const int*)d_in[1], (const u16*)d_in[2], (const u16*)d_in[3],
                                         (const u16*)d_in[4], (const u16*)d_in[5], (u16*)d_out, (unsigned char*)d_ws);
}
```

```cpp
#include <hip/hip_runtime.h>

typedef unsigned short u16;
typedef unsigned int u32;
typedef unsigned long long u64;
typedef u32 v4u __attribute__((ext_vector_type(4)));
typedef u32 v2u __attribute__((ext_vector_type(2)));
typedef float f2 __attribute__((ext_vector_type(2)));
typedef float f4 __attribute__((ext_vector_type(4)));
typedef float f16v __attribute__((ext_vector_type(16)));
typedef __bf16 bf2 __attribute__((ext_vector_type(2)));
typedef __bf16 bf8 __attribute__((ext_vector_type(8)));
typedef short s4 __attribute__((ext_vector_type(4)));
typedef u32 v16u __attribute__((ext_vector_type(16)));
#define LAS __attribute__((address_space(3)))

#define A_W 5
#define B_W0 7
#define B_N1 3
#define B_W1 7
#define WIN_Q 14
#define T_X 480
#define T_A 350
#define T_H 760
#define ROUND_Q 0
#define HELP_U 1
#define HELP_Q 1
#define HELP_PRIO 1
#define CODE_LOADS 2
#define CODE_BYTES 12544

namespace {
constexpr int D = 576, F = 4608;
constexpr int NWG = 252, NMEM = 14, L = 32;
constexpr u32 SENT = 0xFFFFFFFFu;
constexpr int WS_E = 0, WS_ACK = 4096, WS_DONE = 8192, WS_H = 16384, WS_XR = 65536, XR_REP = 9216 + 1024, WS_YP = 196608,
              WS_BYTES = WS_YP + NWG * 4096;
constexpr int OOB = 0x40000000;
constexpr int HS = 1040, XS = 1168;
constexpr int L_H = 64, L_XR = L_H + 4 * HS, L_PART = L_XR + 4 * XS, L_SLAB = L_PART + 12 * 576, L_BYTES = L_SLAB + 7 * 4096;
enum { F_ELO = 0, F_EHI = 1, F_RED = 2, F_XR = 3, F_H = 4, F_GO = 5, F_ISS = 6, F_GO2 = 7 };
constexpr u32 G_SPINS = 1u << 15, L_SPINS = 1u << 21;
constexpr int AUX_NT = 2, AUX_SC1 = 16;

typedef __amdgpu_buffer_rsrc_t rsrc_t;
__device__ __forceinline__ rsrc_t mk(const void* p, int bytes) { return __builtin_amdgcn_make_buffer_rsrc((void*)p, 0, bytes, 0x00020000); }
#define SB() __builtin_amdgcn_sched_barrier(0)
#define WAIT_VM(n) asm volatile("s_waitcnt vmcnt(%0)" ::"i"(n) : "memory")
#define WAIT_LGKM() asm volatile("s_waitcnt lgkmcnt(0)" ::: "memory")
#define CBAR() asm volatile("" ::: "memory")

__device__ __forceinline__ u32 lds_ld(LAS unsigned char* l, int w) { return __hip_atomic_load((LAS u32*)l + w, __ATOMIC_RELAXED, __HIP_MEMORY_SCOPE_WORKGROUP); }
__device__ __forceinline__ void lds_st(LAS unsigned char* l, int w, u32 v) { __hip_atomic_store((LAS u32*)l + w, v, __ATOMIC_RELAXED, __HIP_MEMORY_SCOPE_WORKGROUP); }
__device__ __forceinline__ u32 lds_inc(LAS unsigned char* l, int w, int lane) {
    u32 old = 0;
    if (lane == 0) old = __builtin_amdgcn_atomic_inc32((LAS u32*)l + w, 0xFFFFFFFFu, __ATOMIC_RELAXED, "workgroup");
    return __builtin_amdgcn_readfirstlane(old);
}
__device__ __forceinline__ void lds_wait(LAS unsigned char* l, int w, u32 target) {
#pragma nounroll
    for (u32 i = 0; i < L_SPINS; ++i) {
        if (__builtin_amdgcn_readfirstlane(lds_ld(l, w)) >= target) break;
        __builtin_amdgcn_s_sleep(1);
    }
    CBAR();
}
__device__ __forceinline__ u32 pk_bf16(float a, float b) {
    bf2 r = __builtin_convertvector(f2{a, b}, bf2);
    return __builtin_bit_cast(u32, r);
}
__device__ __forceinline__ float rnd_bf16(float a) { return __builtin_bit_cast(float, pk_bf16(0.f, a)); }
__device__ __forceinline__ float lo_f(u32 v) { return __builtin_bit_cast(float, v << 16); }
__device__ __forceinline__ float hi_f(u32 v) { return __builtin_bit_cast(float, v & 0xFFFF0000u); }
__device__ __forceinline__ u32 umin(u32 a, u32 b) { return a < b ? a : b; }
template <int CTRL> __device__ __forceinline__ u32 dpp(u32 v) { return __builtin_amdgcn_update_dpp(0u, v, CTRL, 0xF, 0xF, true); }
template <int NA, int NB> __device__ __forceinline__ void wait_vm2(int longv) {
    if (NA == NB) WAIT_VM(NA);
    else if (NA < NB) asm volatile("s_cmp_eq_u32 %0, 7\n\ts_cbranch_scc1 1f\n\ts_waitcnt vmcnt(%1)\n1:\n\ts_waitcnt vmcnt(%2)" ::"s"(longv), "i"(NA), "i"(NB) : "memory", "scc");
    else asm volatile("s_cmp_lg_u32 %0, 7\n\ts_cbranch_scc1 1f\n\ts_waitcnt vmcnt(%2)\n1:\n\ts_waitcnt vmcnt(%1)" ::"s"(longv), "i"(NA), "i"(NB) : "memory", "scc");
}
__device__ __forceinline__ void pace(u32 t0, u32 due) {
    asm volatile("s_branch 2f\n1:\n\ts_sleep 1\n2:\n\ts_memrealtime vcc\n\ts_waitcnt lgkmcnt(0)\n\ts_sub_u32 vcc_lo, vcc_lo, %0\n\t"
                 "s_cmp_lt_u32 vcc_lo, %1\n\ts_cbranch_scc1 1b" ::"s"(t0), "s"(due) : "vcc", "scc", "memory");
}
template <int I> struct Issue {
    static __device__ __forceinline__ void run(v4u (&a)[L], rsrc_t rv, int voff, int longv) {
        constexpr int WB = I < B_N1 ? B_W0 : B_W1;
        constexpr int NA = A_W - 1 < I ? A_W - 1 : 63, NB = WB - 1 < I ? WB - 1 : 63;
        if (NA < 63 || NB < 63) wait_vm2<NA, NB>(longv);
        SB();
        a[I] = __builtin_amdgcn_raw_buffer_load_b128(rv, voff, I * (4 * F), AUX_NT);
        SB();
        Issue<I + 1>::run(a, rv, voff, longv);
    }
};
template <> struct Issue<L> {
    static __device__ __forceinline__ void run(v4u (&)[L], rsrc_t, int, int) {}
};
__device__ __forceinline__ u32 start_time() {
    u32 t;
    asm volatile("s_mov_b32 %0, s82" : "=s"(t));
    return t;
}
template <int I> __device__ __forceinline__ unsigned char* arg() {
    u64 p;
    asm volatile("s_mov_b64 %0, s[%1:%2]" : "=s"(p) : "i"(84 + 2 * I), "i"(85 + 2 * I));
    return (unsigned char*)p;
}

__device__ __forceinline__ u64 now() { u64 t; asm volatile("s_memrealtime %0\n\ts_waitcnt lgkmcnt(0)" : "=s"(t)::"memory"); return t; }
#define ST(i) do {} while (0)
#define STC(i) do {} while (0)
#define ST_DECL
#define ST_OUT() do {} while (0)
}

__global__ void __launch_bounds__(512, 2)
quip_decode(const u16* x_, const int* qw_, const u16* sc_, const u16* zr_, const u16* U_, const u16* V_, u16* y_, unsigned char* ws_) {
    __shared__ __attribute__((aligned(16))) unsigned char smem[L_BYTES];
    LAS unsigned char* lds = (LAS unsigned char*)smem;
    ST_DECL;
    ST(0);
    asm volatile("s_load_dwordx16 s[84:99], %0, 0x0\n\ts_memrealtime s[82:83]" ::"s"(__builtin_amdgcn_kernarg_segment_ptr())
                 : "s82", "s83", "s84", "s85", "s86", "s87", "s88", "s89", "s90", "s91", "s92", "s93", "s94", "s95", "s96", "s97", "s98", "s99");
#define SYNC() asm volatile("s_waitcnt lgkmcnt(0)\n\ts_barrier" ::: "memory")
#define ARG(i) arg<i>()
    const int lane = threadIdx.x & 63, w = __builtin_amdgcn_readfirstlane(threadIdx.x >> 6);
    u32 pf[CODE_LOADS];
    if (w == 4) {
        u64 pc;
        asm volatile("s_getpc_b64 %0" : "=s"(pc));
        const rsrc_t rc = mk((const void*)(pc & ~255ull), CODE_BYTES);
#pragma unroll
        for (int i = 0; i < CODE_LOADS; ++i) pf[i] = __builtin_amdgcn_raw_buffer_load_b32(rc, lane * 128, i * 8192, 0);
        SB();
    }
    const int bx = blockIdx.x;
    const int xx = bx & 7, yy = bx >> 3;
    const int y2 = xx < 3 ? yy - 6 : yy;
    const int tile = 18 * xx + y2;
    const int vi = y2 - 18 + (xx < 3 ? 8 * xx : xx == 3 ? 24 : 13 * xx - 14);
    const int vd = (vi * 3641) >> 16;
    const int c = y2 < 0 ? 6 * xx + yy : y2 < 18 ? y2 : vi - 18 * vd, m = y2 < 0 ? 0 : y2 < 18 ? 1 + xx : 9 + vd;
    if (w == 4 && lane < 16) lds_st(lds, lane, 0);
    const int longv = m >= 9;
    const int nband = longv ? 7 : m == 0 ? 5 : 4;
    const int band0 = longv ? 7 * m - 26 : m == 0 ? 0 : 4 * m + 1;
    const int slot = c * NMEM + m;
#define EPOCH_PTR ((const __attribute__((address_space(4))) u64*)(u64)ARG(7))

    if (w == 4) {
        __builtin_amdgcn_s_setprio(3);
        const v4u sent = {SENT, SENT, SENT, SENT};
        int ho = (nband == 4 && lane < 16) ? WS_H + tile * 256 + lane * 16 : OOB;
        asm volatile("" : "+v"(ho));
        SYNC();
        const rsrc_t rw = mk(ARG(7), WS_BYTES);
        __builtin_amdgcn_raw_buffer_store_b128(sent, rw, ho, 0, AUX_SC1);
        const u64 e0 = *EPOCH_PTR;
        SB();
        const u32 t0 = start_time();
        WAIT_VM(0);
        SB();
#pragma unroll
        for (int i = 0; i < CODE_LOADS; ++i) asm volatile("" ::"v"(pf[i]));
        const u64 e1 = e0 + 1;
        const u32 e1lo = (u32)e1, e1hi = (u32)(e1 >> 32);
        __builtin_amdgcn_raw_buffer_store_b64(v2u{e1lo, e1hi}, rw, lane == 0 ? WS_ACK + c * 128 + m * 8 : (lane == 1 && nband == 4) ? WS_DONE + tile * 8 : OOB, 0, AUX_SC1);
        lds_st(lds, F_ELO, e1lo);
        ST(1);
        if (nband == 4) {
            const int xo = WS_XR + xx * XR_REP + lane * 16;
            lds_wait(lds, F_ISS, 3);
            pace(t0, T_X);
#pragma nounroll
            for (u32 it = 0; it < G_SPINS; ++it) {
                CBAR();
                v4u g[9];
                bool ok = true;
#pragma unroll
                for (int k = 0; k < 9; ++k) g[k] = __builtin_amdgcn_raw_buffer_load_b128(rw, xo, k * 1024, AUX_SC1);
#pragma unroll
                for (int k = 0; k < 9; ++k) ok = ok && g[k].y == e1lo && g[k].w == e1lo;
                STC(6);
                if (__all(ok) || it == G_SPINS - 1) {
                    const int o = L_XR + ((lane >> 3) & 3) * XS + (lane >> 5) * 64 + (lane & 7) * 8;
#pragma unroll
                    for (int k = 0; k < 9; ++k) *(LAS v2u*)(lds + o + k * 128) = v2u{g[k].x, g[k].z};
                    break;
                }
            }
            WAIT_LGKM();
            lds_st(lds, F_XR, 1);
        }
        ST(2);
        const int nt = 2 * nband, j0 = 2 * band0;
        pace(t0, T_A);
        {
            const int ao = WS_DONE + (j0 + (lane < nt ? lane : 0)) * 8;
#pragma nounroll
            for (u32 it = 0; it < G_SPINS; ++it) {
                CBAR();
                const v2u a = __builtin_amdgcn_raw_buffer_load_b64(rw, ao, 0, AUX_SC1);
                STC(7);
                if (__all(a.x == e1lo && a.y == e1hi)) break;
                __builtin_amdgcn_s_sleep(2);
            }
        }
        pace(t0, T_H);
        ST(3);
#pragma nounroll
        for (u32 it = 0; it < G_SPINS; ++it) {
            CBAR();
            v4u g[4];
            bool ok = true;
#pragma unroll
            for (int k = 0; k < 4; ++k)
                g[k] = __builtin_amdgcn_raw_buffer_load_b128(rw, 4 * k + (lane >> 4) < nt ? WS_H + j0 * 256 + k * 1024 + lane * 16 : OOB, 0, AUX_SC1);
#pragma unroll
            for (int k = 0; k < 4; ++k) ok = ok && g[k].x != SENT && g[k].y != SENT && g[k].z != SENT && g[k].w != SENT;
            STC(5);
            if (__all(ok) || it == G_SPINS - 1) {
                const int o = L_H + ((lane >> 2) & 3) * HS + (lane >> 4) * 64 + (lane & 3) * 16;
#pragma unroll
                for (int k = 0; k < 4; ++k) *(LAS v4u*)(lds + o + k * 256) = g[k];
                break;
            }
        }
        WAIT_LGKM();
        lds_st(lds, F_H, 1);
        ST(4);
        if (bx == 0) {
#pragma nounroll
            for (u32 it = 0; it < G_SPINS; ++it) {
                CBAR();
                bool ok = true;
#pragma unroll
                for (int k = 0; k < 3; ++k) {
                    const int a = (k * 64 + lane < 144 && (lane & 7) != 7) ? k * 64 + lane : 0;
                    const v4u v = __builtin_amdgcn_raw_buffer_load_b128(rw, WS_ACK + a * 16, 0, AUX_SC1);
                    ok = ok && v.x == e1lo && v.y == e1hi && v.z == e1lo && v.w == e1hi;
                }
                if (__all(ok)) break;
            }
            CBAR();
            __builtin_amdgcn_raw_buffer_store_b64(v2u{e1lo, e1hi}, rw, lane == 0 ? WS_E : OOB, 0, AUX_SC1);
        }
        ST_OUT();
        return;
    }

    if (w > 4 && !longv) {
        const int wk = w - 5;
        __builtin_amdgcn_s_setprio(2);
        const int q = lane >> 4;
        LAS float* part = (LAS float*)(lds + L_PART);
        if (m == 0) {
            const int s = (lane >> 2) & 3, c4 = lane & 3;
            int uo = (192 * wk + 16 * s + 4 * q) * (D * 2) + c * 64 + c4 * 16;
            int xo = (lane & 3) * (D * 2) + (192 * wk + 16 * ((lane >> 2) & 3) + 4 * q) * 2;
            asm volatile("" : "+v"(uo), "+v"(xo));
            SYNC();
            const rsrc_t ru = mk(ARG(4), D * D * 2), rx = mk(ARG(0), 4 * D * 2), rw = mk(ARG(7), WS_BYTES);
            v4u u[12];
            v2u xa[3];
#pragma unroll
            for (int i = 0; i < 3; ++i) xa[i] = __builtin_amdgcn_raw_buffer_load_b64(rx, xo + i * 128, 0, 0);
#pragma unroll
            for (int i = 0; i < 12; ++i) u[i] = __builtin_amdgcn_raw_buffer_load_b128(ru, uo + (i & 3) * (D * 2), (i >> 2) * 64 * (D * 2), AUX_NT);
            u32 e0;
            asm volatile("s_load_dword %0, %1, 0x0" : "=s"(e0) : "s"(EPOCH_PTR));
            SB();
            ST(1);
            f4 acc[8];
#pragma unroll
            for (int k = 0; k < 8; ++k) acc[k] = f4{0, 0, 0, 0};
#pragma unroll
            for (int bl = 0; bl < 3; ++bl) {
#pragma unroll
                for (int k = 0; k < 8; ++k) {
                    const u32 sel = (k & 1) ? 0x07060302u : 0x05040100u;
                    const v2u bv = {__builtin_amdgcn_perm(u[4 * bl + 1][k >> 1], u[4 * bl][k >> 1], sel), __builtin_amdgcn_perm(u[4 * bl + 3][k >> 1], u[4 * bl + 2][k >> 1], sel)};
                    acc[k] = __builtin_amdgcn_mfma_f32_16x16x16bf16_1k(__builtin_bit_cast(s4, xa[bl]), __builtin_bit_cast(s4, bv), acc[k], 0, 0, 0);
                }
            }
            ST(2);
            if (q == s) {
#pragma unroll
                for (int k = 0; k < 8; ++k) *(LAS f4*)(part + (4 * wk + s) * 144 + c4 * 36 + k * 4) = acc[k];
            }
            asm volatile("s_waitcnt lgkmcnt(0)" : "+s"(e0)::"memory");
            if (lds_inc(lds, F_RED, lane) == 2) {
                CBAR();
                f2 t = {0, 0};
#pragma unroll
                for (int p = 0; p < 12; ++p) {
                    LAS const float* pp = part + p * 144 + ((lane >> 2) & 3) * 36 + (lane & 3) * 8 + q;
                    t += f2{pp[0], pp[4]};
                }
                const u32 d = pk_bf16(t.x, t.y);
                const v4u o = {dpp<0xA0>(d), e0 + 1, dpp<0xF5>(d), e0 + 1};
#pragma unroll
                for (int k = 0; k < 4; ++k)
                    __builtin_amdgcn_raw_buffer_store_b128(o, rw, WS_XR + (lane & 1) * XR_REP + c * 512 + (lane >> 1) * 16, 2 * k * XR_REP, AUX_SC1);
                SB();
                lds_st(lds, F_GO, 1);
                ST(3);
            }
            if (wk != 0) { ST_OUT(); return; }
            __builtin_amdgcn_s_setprio(0);
        } else {
            const int s = (lane >> 3) & 1, cq = lane & 7;
            int qo = (192 * wk + 32 * s + 8 * q) * (F * 4) + tile * 128 + cq * 16;
            int so = (6 * wk + s) * (F * 2) + tile * 64 + cq * 8;
            asm volatile("" : "+v"(qo), "+v"(so));
            SYNC();
            const rsrc_t rq = mk(ARG(1), D * F * 4), rs = mk(ARG(2), 18 * F * 2), rz = mk(ARG(3), 18 * F * 2), rw = mk(ARG(7), WS_BYTES);
            v4u qv[24];
            v2u sv[3], zv[3];
#pragma unroll
            for (int i = 0; i < 3; ++i) {
                sv[i] = __builtin_amdgcn_raw_buffer_load_b64(rs, so, i * 2 * (F * 2), AUX_NT);
                zv[i] = __builtin_amdgcn_raw_buffer_load_b64(rz, so, i * 2 * (F * 2), AUX_NT);
            }
#pragma unroll
            for (int i = 0; i < WIN_Q; ++i) qv[i] = __builtin_amdgcn_raw_buffer_load_b128(rq, qo, ((i >> 3) * 64 + (i & 7)) * (F * 4), AUX_NT);
            SB();
            v4u bw[3][4];
#pragma unroll
            for (int p = 0; p < 12; ++p) {
                const int bl = p >> 2, t = p & 3, i = 2 * p + WIN_Q;
                if (i < 24) {
                    WAIT_VM(WIN_Q - 2);
                    qv[i] = __builtin_amdgcn_raw_buffer_load_b128(rq, qo, ((i >> 3) * 64 + (i & 7)) * (F * 4), AUX_NT);
                    qv[i + 1] = __builtin_amdgcn_raw_buffer_load_b128(rq, qo, (((i + 1) >> 3) * 64 + ((i + 1) & 7)) * (F * 4), AUX_NT);
                    SB();
                    if (i + 2 >= 24) {
                        if (lds_inc(lds, F_ISS, lane) == 2) lds_st(lds, F_GO, 1);
                        ST(1);
                    }
                }
                const float zf[4] = {lo_f(zv[bl].x), hi_f(zv[bl].x), lo_f(zv[bl].y), hi_f(zv[bl].y)};
                const float sf[4] = {lo_f(sv[bl].x), hi_f(sv[bl].x), lo_f(sv[bl].y), hi_f(sv[bl].y)};
                f2 d[4];
                u32 r[4];
#pragma unroll
                for (int k = 0; k < 4; ++k) {
                    d[k] = f2{(float)(int)qv[2 * p][k], (float)(int)qv[2 * p + 1][k]};
                }
#pragma unroll
                for (int k = 0; k < 4; ++k) d[k] -= f2{zf[k], zf[k]};
#pragma unroll
                for (int k = 0; k < 4; ++k) d[k] = f2{rnd_bf16(d[k].x), rnd_bf16(d[k].y)};
#pragma unroll
                for (int k = 0; k < 4; ++k) d[k] *= f2{sf[k], sf[k]};
#pragma unroll
                for (int k = 0; k < 4; ++k) r[k] = pk_bf16(d[k].x, d[k].y);
                asm volatile("" : "+v"(r[0]), "+v"(r[1]), "+v"(r[2]), "+v"(r[3]));
#pragma unroll
                for (int k = 0; k < 4; ++k) bw[bl][k][t] = r[k];
                SB();
            }
            ST(2);
            lds_wait(lds, F_XR, 1);
            ST(3);
            f4 acc[4];
#pragma unroll
            for (int k = 0; k < 4; ++k) acc[k] = f4{0, 0, 0, 0};
            const int ao = L_XR + (lane & 3) * XS + (192 * wk + 32 * ((lane >> 2) & 1) + 8 * q) * 2;
#pragma unroll
            for (int bl = 0; bl < 3; ++bl) {
                const v4u A = *(LAS const v4u*)(lds + ao + bl * 128);
#pragma unroll
                for (int k = 0; k < 4; ++k)
                    acc[k] = __builtin_amdgcn_mfma_f32_16x16x32_bf16(__builtin_bit_cast(bf8, A), __builtin_bit_cast(bf8, bw[bl][k]), acc[k], 0, 0, 0);
            }
            if (q == s) {
#pragma unroll
                for (int k = 0; k < 4; ++k) *(LAS f4*)(part + (2 * wk + s) * 160 + cq * 20 + k * 4) = acc[k];
            }
            WAIT_LGKM();
            if (lds_inc(lds, F_RED, lane) == 2) {
                CBAR();
                f2 t = {0, 0};
#pragma unroll
                for (int p = 0; p < 6; ++p) {
                    LAS const float* pp = part + p * 160 + ((lane >> 1) & 7) * 20 + (lane & 1) * 8 + q;
                    t += f2{pp[0], pp[4]};
                }
                const u32 d = umin(pk_bf16(t.x, t.y), SENT - 1);
                const v4u o = {d, dpp<0x55>(d), dpp<0xAA>(d), dpp<0xFF>(d)};
                __builtin_amdgcn_raw_buffer_store_b128(o, rw, (lane & 3) ? OOB : WS_H + tile * 256 + lane * 4, 0, AUX_SC1);
                SB();
                ST(4);
            }
            ST_OUT();
            return;
        }
    }

    const int vw = w > 4 ? w - 1 : w;
    int voff = ((band0 + vw) * 64 + (lane >> 5)) * (F * 2) + c * 512 + (lane & 31) * 16;
    v4u up[3] = {{0, 0, 0, 0}, {0, 0, 0, 0}, {0, 0, 0, 0}};
    if (w < 4 || longv) {
        const bool help = HELP_U && xx < 3 && !longv && m != 0;
        const int t3 = ((3 - 2 * xx) & 7) + 8 * (vw >> 1);
        const bool helpq = HELP_Q && longv && y2 < 26 && t3 < 18;
        int uo = (lane >> 3) * (longv ? F * 4 : D * 2) + (lane & 7) * 16 +
                 (longv ? (184 - 16 * (y2 - 18) - 8 * (vw & 1)) * (F * 4) + (18 * xx + t3) * 128 : (y2 * 4 + w) * (8 * D * 2) + xx * 384);
        asm volatile("" : "+v"(voff), "+v"(uo));
        if (HELP_PRIO) __builtin_amdgcn_s_setprio(3);
        SYNC();
        if (help) {
            const rsrc_t ru = mk(ARG(4), D * D * 2);
#pragma unroll
            for (int k = 0; k < 3; ++k) up[k] = __builtin_amdgcn_raw_buffer_load_b128(ru, uo + k * 128, 0, AUX_NT);
            SB();
        }
        if (helpq) {
            const rsrc_t rq = mk(ARG(1), D * F * 4);
#pragma unroll
            for (int k = 0; k < 3; ++k) up[k] = __builtin_amdgcn_raw_buffer_load_b128(rq, uo, k * 192 * (F * 4), AUX_NT);
            SB();
        }
        if (HELP_PRIO) __builtin_amdgcn_s_setprio(0);
    }
    const rsrc_t rv = mk(ARG(5), F * F * 2), rw = mk(ARG(7), WS_BYTES);
    if (!longv) lds_wait(lds, F_GO, 1);
    ST(4);
    v4u a[L];
    Issue<0>::run(a, rv, voff, __builtin_amdgcn_readfirstlane(nband));
    const u32 two = (lane & 32) ? 0x0302u : 0x0100u;
    const u32 one = (lane & 1) ? ((two << 16) | 0x0C0Cu) : (0x0C0C0000u | two);
    const int jj = (lane & 7) >> 1;
    u32 s0 = jj == 0 ? one : 0x0C0C0C0Cu, s1 = jj == 1 ? one : 0x0C0C0C0Cu, s2 = jj == 2 ? one : 0x0C0C0C0Cu, s3 = jj == 3 ? one : 0x0C0C0C0Cu;
    asm volatile("" : "+v"(s0), "+v"(s1), "+v"(s2), "+v"(s3) : "v"(up[0]), "v"(up[1]), "v"(up[2]));
    ST(5);
    lds_wait(lds, F_H, 1);
    ST(6);
    const u32 tag = lds_ld(lds, F_ELO);
    f16v acc;
#pragma unroll
    for (int i = 0; i < 16; ++i) acc[i] = 0.f;
    {
        LAS const v4u* hp = (LAS const v4u*)(lds + L_H + ((lane >> 3) & 3) * HS + vw * 128);
        v4u hv[L / 4];
#pragma unroll
        for (int t = 0; t < L / 4; ++t) hv[t] = hp[t];
        SB();
#pragma unroll
        for (int i = 0; i < L; ++i) {
            const u32 hh = hv[i >> 2][i & 3];
            const v4u A = {__builtin_amdgcn_perm(hh, hh, s0), __builtin_amdgcn_perm(hh, hh, s1), __builtin_amdgcn_perm(hh, hh, s2), __builtin_amdgcn_perm(hh, hh, s3)};
            acc = __builtin_amdgcn_mfma_f32_32x32x16_bf16(__builtin_bit_cast(bf8, A), __builtin_bit_cast(bf8, a[i]), acc, 0, 0, 0);
        }
    }
    ST(1);
    {
        LAS f4* slab = (LAS f4*)(lds + L_SLAB);
        const int p = 2 * (lane & 31) + (lane >> 5);
#pragma unroll
        for (int b = 0; b < 4; ++b) slab[vw * 256 + b * 64 + p] = f4{acc[4 * b], acc[4 * b + 1], acc[4 * b + 2], acc[4 * b + 3]};
        WAIT_LGKM();
        __builtin_amdgcn_s_barrier();
        CBAR();
        if (w > 4) { ST_OUT(); return; }
        LAS const f4* sp = slab + w * 64 + lane;
        f4 t = (sp[0] + sp[256]) + (sp[512] + sp[768]);
        if (nband > 4) t += sp[1024];
        if (nband > 5) t += sp[1280] + sp[1536];
        const v4u u = __builtin_bit_cast(v4u, t) + 0x80u;
        const v4u o = {__builtin_amdgcn_perm(u.x, tag, 0x07060500u), __builtin_amdgcn_perm(u.y, tag, 0x07060501u),
                       __builtin_amdgcn_perm(u.z, tag, 0x07060502u), __builtin_amdgcn_perm(u.w, tag, 0x07060503u)};
        __builtin_amdgcn_raw_buffer_store_b128(o, rw, WS_YP + slot * 4096 + w * 1024 + lane * 16, 0, AUX_SC1);
    }
    ST(2);
    const int sl = m + NMEM * w;
    if (sl < 16) {
        const int g = lane >> 4;
        const int base = WS_YP + (c * NMEM + g) * 4096 + sl * 256 + (lane & 15) * 16;
        v4u p[4];
#pragma nounroll
        for (u32 it = 0; it < G_SPINS; ++it) {
            CBAR();
            bool ok = true;
#pragma unroll
            for (int k = 0; k < 4; ++k) p[k] = __builtin_amdgcn_raw_buffer_load_b128(rw, 4 * k + g < NMEM ? base + k * 4 * 4096 : OOB, 0, AUX_SC1);
#pragma unroll
            for (int k = 0; k < 4; ++k) {
                const u32 got = __builtin_amdgcn_perm(__builtin_amdgcn_perm(p[k].w, p[k].z, 0x04000c0cu), __builtin_amdgcn_perm(p[k].y, p[k].x, 0x0c0c0400u), 0x07060100u);
                ok = ok && (got == tag || (k == 3 && g >= NMEM - 12));
            }
            if (__all(ok)) break;
        }
#pragma unroll
        for (int k = 0; k < 4; ++k) p[k] &= 0xFFFFFF00u;
        f4 t = (__builtin_bit_cast(f4, p[0]) + __builtin_bit_cast(f4, p[1])) + (__builtin_bit_cast(f4, p[2]) + __builtin_bit_cast(f4, p[3]));
#pragma unroll
        for (int i = 0; i < 4; ++i) {
            const float ti = t[i];
            const u32 v = __builtin_bit_cast(u32, ti);
            const auto r = __builtin_amdgcn_permlane16_swap(v, v, false, false);
            const u32 v2 = __builtin_bit_cast(u32, __builtin_bit_cast(float, (u32)r[0]) + __builtin_bit_cast(float, (u32)r[1]));
            const auto r2 = __builtin_amdgcn_permlane32_swap(v2, v2, false, false);
            t[i] = __builtin_bit_cast(float, (u32)r2[0]) + __builtin_bit_cast(float, (u32)r2[1]);
        }
        const rsrc_t ry = mk(ARG(6), 4 * F * 2);
        __builtin_amdgcn_raw_buffer_store_b64(v2u{pk_bf16(t[0], t[1]), pk_bf16(t[2], t[3])}, ry,
                                              lane < 16 ? ((sl >> 2) * F + c * 256 + (sl & 3) * 64 + lane * 4) * 2 : OOB, 0, 0);
    }
    ST(3);
    ST_OUT();
}

extern "C" void kernel_launch(void* const* d_in, const int* in_sizes, int n_in, void* d_out, int out_size, void* d_ws, size_t ws_size,
                              hipStream_t stream) {
    quip_decode<<<NWG, 512, 0, stream>>>((const u16*)d_in[0], (const int*)d_in[1], (const u16*)d_in[2], (const u16*)d_in[3],
                                         (const u16*)d_in[4], (const u16*)d_in[5], (u16*)d_out, (unsigned char*)d_ws);
}
```
